```python
import torch

def _softmax(x, axis=-1):
    return torch.softmax(x, dim=axis)
def _gelu(x):
    return torch.nn.functional.gelu(x, approximate="tanh")
def _silu(x):
    return torch.nn.functional.silu(x)
def _sigmoid(x):
    return torch.sigmoid(x)
def _layernorm(x, axis=-1, eps=1e-5):
    m = x.mean(dim=axis, keepdim=True)
    v = x.var(dim=axis, keepdim=True, unbiased=False)
    return (x - m) / torch.sqrt(v + eps)
def _scatter_add(tgt, idx, src, axis=0):
    return tgt.clone().index_add_(axis, idx, src)
def _segment_sum(src, seg, n):
    out = torch.zeros((n,) + src.shape[1:], dtype=src.dtype)
    return out.index_add_(0, seg, src)
def _pad(x, pw):
    flat = tuple(v for pair in reversed(pw) for v in pair)
    return torch.nn.functional.pad(x, flat)

kernel_name = 'standalone__fused_io_aware_attn__prompt'

def setup_inputs():

    _w = (lambda a: (lambda t: t.to(torch.bfloat16) if t.is_floating_point() else t)(torch.as_tensor(a)))
    B, H, S = 1, 16, 4096
    Dh = max(1, 448 // 16)
    return {
        "q": _w((lambda *s: torch.randn(*s, dtype=torch.bfloat16) * 0.8)(B, H, S, Dh)),
        "k": _w((lambda *s: torch.randn(*s, dtype=torch.bfloat16) * 0.8)(B, H, S, Dh)),
        "v": _w((lambda *s: torch.randn(*s, dtype=torch.bfloat16) * 0.05)(B, H, S, Dh)),
    }

def reference(q, k, v):
    scale = q.shape[-1] ** -0.5
    s = torch.einsum('bhqd,bhkd->bhqk', q, k) * scale
    p = _softmax(s, axis=-1)
    return torch.einsum('bhqk,bhkd->bhqd', p, v)
```

```cpp
#include <hip/hip_runtime.h>
#include <hip/hip_bf16.h>
#include <stdint.h>

typedef float    f32x16 __attribute__((ext_vector_type(16)));
typedef float    f32x2  __attribute__((ext_vector_type(2)));
typedef unsigned u32x4  __attribute__((ext_vector_type(4)));
typedef unsigned u32x2  __attribute__((ext_vector_type(2)));
typedef __bf16   bf16x8 __attribute__((ext_vector_type(8)));
typedef __bf16   bf16x2 __attribute__((ext_vector_type(2)));
typedef unsigned short u16;
#define DI __device__ __forceinline__

constexpr int NH = 16, SEQ = 4096, HD = 28, ROWB = HD * 2;
constexpr int PIECE = 128 * ROWB;
constexpr int SLOT = 4 * PIECE;
constexpr int ONES = 4 * SLOT;
constexpr int LDS_BYTES = ONES + 8192;
constexpr int NSTAGE = 16;
constexpr int TENSOR_BYTES = NH * SEQ * ROWB;
constexpr float QSCALE = 0.2726437354213113f;

DI unsigned cvtpk(float lo, float hi) { f32x2 v = {lo, hi}; return __builtin_bit_cast(unsigned, __builtin_convertvector(v, bf16x2)); }
DI float blo(unsigned w) { return __uint_as_float(w << 16); }
DI float bhi(unsigned w) { return __uint_as_float(w & 0xffff0000u); }
DI unsigned scale2(unsigned w) { return cvtpk(blo(w) * QSCALE, bhi(w) * QSCALE); }
DI float upper_half(float x) {
    auto r = __builtin_amdgcn_permlane32_swap(__float_as_uint(x), __float_as_uint(x), false, false);
    return __uint_as_float(r[1]);
}
#define MFMA(a, b, c) __builtin_amdgcn_mfma_f32_32x32x16_bf16(__builtin_bit_cast(bf16x8, a), __builtin_bit_cast(bf16x8, b), c, 0, 0, 0)

DI f32x16 attn_rows_true_max(const u16* __restrict__ Qh, const u16* __restrict__ Kh, const u16* __restrict__ Vh, int row0, int lane) {
    const int r = lane & 31, h = lane >> 5;
    const unsigned keep = h ? 0u : 0xffffffffu;
    const char* qp = (const char*)Qh + (row0 + r) * ROWB + 16 * h;
    const u32x2 qa = *(const u32x2*)(qp), qb = *(const u32x2*)(qp + 8), qc = *(const u32x2*)(qp + 32), qd = *(const u32x2*)(qp + (h ? 32 : 40));
    const u32x4 q0 = {scale2(qa[0]), scale2(qa[1]), scale2(qb[0]), scale2(qb[1])};
    const u32x4 q1 = {scale2(qc[0]), scale2(qc[1]), scale2(qd[0]) & keep, scale2(qd[1]) & keep};
    f32x16 o, zero;
#pragma unroll
    for (int i = 0; i < 16; ++i) { o[i] = 0.f; zero[i] = 0.f; }
    float m = -INFINITY;
    for (int t = 0; t < SEQ / 32; ++t) {
        const char* kp = (const char*)Kh + (t * 32 + r) * ROWB + 16 * h;
        const u32x2 a0 = *(const u32x2*)(kp), a1 = *(const u32x2*)(kp + 8), b0 = *(const u32x2*)(kp + 32), b1 = *(const u32x2*)(kp + (h ? 32 : 40));
        const u32x4 k0 = {a0[0], a0[1], a1[0], a1[1]}, k1 = {b0[0], b0[1], b1[0] & keep, b1[1] & keep};
        f32x16 s = MFMA(k0, q0, zero);
        s = MFMA(k1, q1, s);
        float tm = s[0];
#pragma unroll
        for (int i = 1; i < 16; ++i) tm = fmaxf(tm, s[i]);
        auto sw = __builtin_amdgcn_permlane32_swap(__float_as_uint(tm), __float_as_uint(tm), false, false);
        const float mn = fmaxf(m, fmaxf(__uint_as_float(sw[0]), __uint_as_float(sw[1])));
        const float alpha = __builtin_amdgcn_exp2f(m - mn);
        m = mn;
#pragma unroll
        for (int i = 0; i < 16; ++i) o[i] *= alpha;
        u32x4 p[2];
#pragma unroll
        for (int i = 0; i < 8; ++i) p[i >> 2][i & 3] = cvtpk(__builtin_amdgcn_exp2f(s[2 * i] - mn), __builtin_amdgcn_exp2f(s[2 * i + 1] - mn));
#pragma unroll
        for (int st = 0; st < 2; ++st) {
            u32x4 vf;
#pragma unroll
            for (int j = 0; j < 8; j += 2) {
                const int key = t * 32 + 16 * st + 8 * (j >> 2) + 4 * h + (j & 3);
                unsigned lo = 0x3f80u, hi = 0x3f80u;
                if (r < HD) { lo = Vh[key * HD + r]; hi = Vh[(key + 1) * HD + r]; }
                vf[j >> 1] = lo | (hi << 16);
            }
            o = MFMA(vf, p[st], o);
        }
    }
    return o;
}

extern "C" __global__ __launch_bounds__(768) void standalone__fused_io_aware_attn__prompt_kernel(
        const u16* __restrict__ Q, const u16* __restrict__ K, const u16* __restrict__ V, u16* __restrict__ O) {
    extern __shared__ __attribute__((aligned(256))) char smem[];
    const int wid = __builtin_amdgcn_readfirstlane(threadIdx.x >> 6);
    const int xcd = blockIdx.x & 7, j = blockIdx.x >> 3;
    const int head = xcd * 2 + (j >> 4), qblk = j & 15;
    const unsigned lds0 = (unsigned)(uintptr_t)smem;

    asm volatile("s_getpc_b64 s[74:75]\n"
                 ".Lattn_pc:\n\t"
                 "s_add_u32 s74, s74, .Lattn_hot_code-.Lattn_pc\n\ts_addc_u32 s75, s75, 0\n\t"
                 "s_min_u32 s76, %0, .Lattn_hot_end-.Lattn_hot_code-1024\n\t"
                 "s_add_u32 s74, s74, s76\n\ts_addc_u32 s75, s75, 0\n\t"
                 "v_mbcnt_lo_u32_b32 v159, -1, 0\n\tv_mbcnt_hi_u32_b32 v159, -1, v159\n\tv_lshlrev_b32 v159, 4, v159\n\t"
                 "global_load_dwordx4 v[160:163], v159, s[74:75]"
                 :: "s"(wid * 1024) : "s74", "s75", "s76", "scc", "v159", "v160", "v161", "v162", "v163", "memory");
    __builtin_amdgcn_sched_barrier(0);
    asm volatile("s_cmp_lt_u32 %[w], 8\n\ts_cbranch_scc1 3f\n\t"
                 "s_bitcmp1_b32 %[w], 1\n\ts_cselect_b32 s76, %[vl], %[kl]\n\ts_cselect_b32 s77, %[vh], %[kh]\n\t"
                 "s_cselect_b32 s80, %[lv], %[lk]\n\t"
                 "s_add_u32 s76, s76, %[ho]\n\ts_addc_u32 s77, s77, 0\n\ts_and_b32 s77, s77, 0xffff\n\t"
                 "s_mov_b32 s78, %[nb]\n\ts_mov_b32 s79, 0x20000\n\t"
                 "s_bitcmp1_b32 %[w], 0\n\ts_cselect_b32 s81, %[g1], 0\n\ts_cselect_b32 s82, %[l1], 0\n\t"
                 "s_add_u32 m0, s80, s82\n\t"
                 "v_mbcnt_lo_u32_b32 v159, -1, 0\n\tv_mbcnt_hi_u32_b32 v159, -1, v159\n\tv_lshlrev_b32 v159, 4, v159\n\t"
                 "buffer_load_dwordx4 v159, s[76:79], s81 offen lds\n\t"
                 "buffer_load_dwordx4 v159, s[76:79], s81 offen offset:1024 lds\n\t"
                 "buffer_load_dwordx4 v159, s[76:79], s81 offen offset:2048 lds\n\t"
                 "buffer_load_dwordx4 v159, s[76:79], s81 offen offset:3072 lds\n"
                 "3:"
                 :: [w] "s"(wid), [kl] "s"((unsigned)(uintptr_t)K), [kh] "s"((unsigned)((uintptr_t)K >> 32)), [vl] "s"((unsigned)(uintptr_t)V),
                    [vh] "s"((unsigned)((uintptr_t)V >> 32)), [ho] "s"(head * SEQ * ROWB), [nb] "n"(SEQ * ROWB), [lk] "s"(lds0), [lv] "s"(lds0 + PIECE),
                    [g1] "s"(2048 * ROWB), [l1] "s"(2 * PIECE),
                    "s"(Q), "s"(O)
                 : "s76", "s77", "s78", "s79", "s80", "s81", "s82", "scc", "v159", "memory");
    __builtin_amdgcn_sched_barrier(0);
    if (wid >= 8) {
        const int role = wid - 8, half = role & 1;
        const u16* src = (role & 2) ? V : K;
        const __amdgpu_buffer_rsrc_t srd = __builtin_amdgcn_make_buffer_rsrc((void*)(src + (size_t)head * SEQ * HD), (short)0, SEQ * ROWB, 0x00020000);
        unsigned voff;
        asm volatile("v_mbcnt_lo_u32_b32 %0, -1, 0\n\tv_mbcnt_hi_u32_b32 %0, -1, %0\n\tv_lshlrev_b32 %0, 4, %0" : "=v"(voff));
        asm volatile(
            "s_add_u32 m0, %[l0], 4096\n"
            "s_add_u32 s73, %[g0], 4096\n"
            "buffer_load_dwordx4 %[vo], %[t], s73 offen offset:0 lds\n"
            "s_nop 1\n"
            "buffer_load_dwordx4 %[vo], %[t], s73 offen offset:1024 lds\n"
            "s_nop 1\n"
            "buffer_load_dwordx4 %[vo], %[t], s73 offen offset:2048 lds\n"
            "s_nop 1\n"
            "s_cmp_lg_u32 %[role], 0\n"
            "s_cbranch_scc1 5f\n"
            "s_waitcnt vmcnt(5)\n"
            "5:\n"
            "s_barrier\n"
            "s_waitcnt vmcnt(3)\n"
            "s_barrier\n"
            "s_mov_b32 s70, 1\n"
            "s_and_b32 s71, s70, 3\n"
            "s_mul_i32 s71, s71, 28672\n"
            "s_add_u32 s71, s71, %[l0]\n"
            "s_mul_i32 s72, s70, 7168\n"
            "s_add_u32 s72, s72, %[g0]\n"
            "s_add_u32 m0, s71, 0\n"
            "s_add_u32 s73, s72, 4096\n"
            "buffer_load_dwordx4 %[vo], %[t], s72 offen offset:0 lds\n"
            "s_nop 1\n"
            "buffer_load_dwordx4 %[vo], %[t], s72 offen offset:1024 lds\n"
            "s_nop 1\n"
            "buffer_load_dwordx4 %[vo], %[t], s72 offen offset:2048 lds\n"
            "s_nop 1\n"
            "buffer_load_dwordx4 %[vo], %[t], s72 offen offset:3072 lds\n"
            "s_nop 1\n"
            "s_add_u32 m0, s71, 4096\n"
            "s_nop 0\n"
            "buffer_load_dwordx4 %[vo], %[t], s73 offen offset:0 lds\n"
            "s_nop 1\n"
            "buffer_load_dwordx4 %[vo], %[t], s73 offen offset:1024 lds\n"
            "s_nop 1\n"
            "buffer_load_dwordx4 %[vo], %[t], s73 offen offset:2048 lds\n"
            "s_nop 1\n"
            "s_waitcnt vmcnt(7)\n"
            "s_barrier\n"
            "s_mov_b32 s70, 2\n"
            "s_and_b32 s71, s70, 3\n"
            "s_mul_i32 s71, s71, 28672\n"
            "s_add_u32 s71, s71, %[l0]\n"
            "s_mul_i32 s72, s70, 7168\n"
            "s_add_u32 s72, s72, %[g0]\n"
            "s_add_u32 m0, s71, 0\n"
            "s_add_u32 s73, s72, 4096\n"
            "buffer_load_dwordx4 %[vo], %[t], s72 offen offset:0 lds\n"
            "s_nop 1\n"
            "buffer_load_dwordx4 %[vo], %[t], s72 offen offset:1024 lds\n"
            "s_nop 1\n"
            "buffer_load_dwordx4 %[vo], %[t], s72 offen offset:2048 lds\n"
            "s_nop 1\n"
            "buffer_load_dwordx4 %[vo], %[t], s72 offen offset:3072 lds\n"
            "s_nop 1\n"
            "s_add_u32 m0, s71, 4096\n"
            "s_nop 0\n"
            "buffer_load_dwordx4 %[vo], %[t], s73 offen offset:0 lds\n"
            "s_nop 1\n"
            "buffer_load_dwordx4 %[vo], %[t], s73 offen offset:1024 lds\n"
            "s_nop 1\n"
            "buffer_load_dwordx4 %[vo], %[t], s73 offen offset:2048 lds\n"
            "s_nop 1\n"
            "s_mov_b32 s70, 3\n"
            "6:\n"
            "s_waitcnt vmcnt(7)\n"
            "s_barrier\n"
            "s_and_b32 s71, s70, 3\n"
            "s_mul_i32 s71, s71, 28672\n"
            "s_add_u32 s71, s71, %[l0]\n"
            "s_mul_i32 s72, s70, 7168\n"
            "s_add_u32 s72, s72, %[g0]\n"
            "s_add_u32 m0, s71, 0\n"
            "s_add_u32 s73, s72, 4096\n"
            "buffer_load_dwordx4 %[vo], %[t], s72 offen offset:0 lds\n"
            "s_nop 1\n"
            "buffer_load_dwordx4 %[vo], %[t], s72 offen offset:1024 lds\n"
            "s_nop 1\n"
            "buffer_load_dwordx4 %[vo], %[t], s72 offen offset:2048 lds\n"
            "s_nop 1\n"
            "buffer_load_dwordx4 %[vo], %[t], s72 offen offset:3072 lds\n"
            "s_nop 1\n"
            "s_add_u32 m0, s71, 4096\n"
            "s_nop 0\n"
            "buffer_load_dwordx4 %[vo], %[t], s73 offen offset:0 lds\n"
            "s_nop 1\n"
            "buffer_load_dwordx4 %[vo], %[t], s73 offen offset:1024 lds\n"
            "s_nop 1\n"
            "buffer_load_dwordx4 %[vo], %[t], s73 offen offset:2048 lds\n"
            "s_nop 1\n"
            "s_add_u32 s70, s70, 1\n"
            "s_cmp_lt_u32 s70, 16\n"
            "s_cbranch_scc1 6b\n"
            "s_waitcnt vmcnt(7)\n"
            "s_barrier\n"
            "s_waitcnt vmcnt(0)\n"
            "s_barrier\n"
            "s_barrier\n"
            :: [vo] "v"(voff), [t] "s"(srd), [l0] "s"(lds0 + ((role & 2) ? PIECE : 0) + half * 2 * PIECE), [g0] "s"(half * 2048u * ROWB), [role] "s"(role)
            : "s70", "s71", "s72", "s73", "scc", "memory");
        return;
    }

    const int g = wid >> 2;
    const int rowB = qblk * 256 + (wid & 3) * 64 + 32 * g;
    const __amdgpu_buffer_rsrc_t srdQ = __builtin_amdgcn_make_buffer_rsrc((void*)Q, (short)0, TENSOR_BYTES, 0x00020000);
    const __amdgpu_buffer_rsrc_t srdO = __builtin_amdgcn_make_buffer_rsrc((void*)O, (short)0, TENSOR_BYTES, 0x00020000);
    int bad;
    asm volatile(
        "; ---- prologue: request Q block B, then LDS addresses and constants under the load latency ----\n"
        "v_mbcnt_lo_u32_b32 v88, -1, 0\n"
        "v_mbcnt_hi_u32_b32 v88, -1, v88\n"
        "v_and_b32 v89, 31, v88\n"
        "v_lshrrev_b32 v90, 5, v88\n"
        "v_mul_u32_u24 v21, 56, v89\n"
        "v_lshl_add_u32 v21, v90, 4, v21\n"
        "buffer_load_dwordx4 v[128:131], v21, %[srdq], %[sob] offen offset:0\n"
        "buffer_load_dwordx4 v[132:135], v21, %[srdq], %[sob] offen offset:32\n"
        "v_mov_b32 v164, 0\n"
        "v_mov_b32 v165, 0\n"
        "v_mov_b32 v166, 0\n"
        "v_mov_b32 v167, 0\n"
        "v_lshlrev_b32 v93, 4, v88\n"
        "v_mov_b32 v96, 0x3f80\n"
        "v_mov_b32 v97, 0\n"
        "v_mfma_f32_32x32x16_bf16 v[24:39], v[164:167], v[164:167], 0\n"
        "v_add_u32 v93, %[sw], v93\n"
        "v_mov_b32 v98, 0x3f80\n"
        "v_mov_b32 v99, 0\n"
        "v_add_u32 v8, %[sg], v21\n"
        "ds_write_b128 v93, v[96:99]\n"
        "v_mfma_f32_32x32x16_bf16 v[40:55], v[164:167], v[164:167], 0\n"
        "v_add_u32 v22, %[sl], v21\n"
        "v_bfe_u32 v91, v88, 2, 2\n"
        "v_mul_u32_u24 v91, 56, v91\n"
        "v_and_b32 v92, 3, v88\n"
        "v_mfma_f32_32x32x16_bf16 v[104:119], v[164:167], v[164:167], 0\n"
        "v_lshl_add_u32 v91, v92, 3, v91\n"
        "v_and_b32 v92, 16, v88\n"
        "v_lshl_add_u32 v91, v92, 1, v91\n"
        "v_mul_u32_u24 v92, 448, v90\n"
        "v_add3_u32 v91, v91, v92, %[sg]\n"
        "v_add_u32 v16, 7168, v91\n"
        "v_mov_b32 v94, 28672\n"
        "v_mov_b32 v95, 28672\n"
        "v_mov_b32 v12, v8\n"
        "v_mov_b32 v23, v22\n"
        "s_add_u32 s41, %[sl], 114688\n"
        "s_mov_b32 exec_lo, 0\n"
        "v_mov_b32 v12, s41\n"
        "v_mov_b32 v23, s41\n"
        "v_mov_b32 v94, 0\n"
        "s_mov_b32 exec_hi, 0x88880000\n"
        "s_add_u32 s41, %[sl], 114848\n"
        "v_mov_b32 v16, s41\n"
        "s_mov_b32 exec_lo, 0x88880000\n"
        "v_mov_b32 v95, 0\n"
        "s_mov_b32 exec_hi, 0\n"
        "s_add_u32 s41, %[sl], 114912\n"
        "v_mov_b32 v16, s41\n"
        "s_mov_b64 exec, -1\n"
        "v_add_u32 v9, 28672, v8\n"
        "v_add_u32 v13, v94, v12\n"
        "v_add_u32 v17, v95, v16\n"
        "v_add_u32 v10, 28672, v9\n"
        "v_add_u32 v14, v94, v13\n"
        "v_add_u32 v18, v95, v17\n"
        "v_add_u32 v11, 28672, v10\n"
        "v_add_u32 v15, v94, v14\n"
        "v_add_u32 v19, v95, v18\n"
        "v_mov_b32 v20, 0xdddddddd\n"
        "v_lshlrev_b32 v5, 4, v88\n"
        "v_lshlrev_b32 v92, 3, v90\n"
        "v_add_u32 v3, %[sxw], v5\n"
        "v_add_u32 v4, %[sxr], v5\n"
        "v_sub_u32 v6, v21, v92\n"
        "v_add_u32 v7, %[sst], v5\n"
        "v_add_u32 v6, %[sst], v6\n"
        "s_mov_b32 s60, 0x3e8b97f6\n"
        "s_mov_b32 s61, 0x3e8b97f6\n"
        "; ---- scale block B of Q and pass a copy to the partner wave (its block A) ----\n"
        "v_add_u32 v100, %[sqw], v5\n"
        "v_add_u32 v101, %[sqr], v5\n"
        "s_waitcnt vmcnt(1)\n"
        "v_lshlrev_b32 v88, 16, v128\n"
        "v_and_b32 v89, 0xffff0000, v128\n"
        "v_pk_mul_f32 v[88:89], v[88:89], s[60:61]\n"
        "v_cvt_pk_bf16_f32 v128, v88, v89\n"
        "v_lshlrev_b32 v90, 16, v129\n"
        "v_and_b32 v91, 0xffff0000, v129\n"
        "v_pk_mul_f32 v[90:91], v[90:91], s[60:61]\n"
        "v_cvt_pk_bf16_f32 v129, v90, v91\n"
        "v_lshlrev_b32 v92, 16, v130\n"
        "v_and_b32 v93, 0xffff0000, v130\n"
        "v_pk_mul_f32 v[92:93], v[92:93], s[60:61]\n"
        "v_cvt_pk_bf16_f32 v130, v92, v93\n"
        "v_lshlrev_b32 v94, 16, v131\n"
        "v_and_b32 v95, 0xffff0000, v131\n"
        "v_pk_mul_f32 v[94:95], v[94:95], s[60:61]\n"
        "v_cvt_pk_bf16_f32 v131, v94, v95\n"
        "ds_write_b128 v100, v[128:131]\n"
        "s_waitcnt vmcnt(0)\n"
        "v_lshlrev_b32 v88, 16, v132\n"
        "v_and_b32 v89, 0xffff0000, v132\n"
        "v_pk_mul_f32 v[88:89], v[88:89], s[60:61]\n"
        "v_cvt_pk_bf16_f32 v132, v88, v89\n"
        "v_lshlrev_b32 v90, 16, v133\n"
        "v_and_b32 v91, 0xffff0000, v133\n"
        "v_pk_mul_f32 v[90:91], v[90:91], s[60:61]\n"
        "v_cvt_pk_bf16_f32 v133, v90, v91\n"
        "v_lshlrev_b32 v92, 16, v134\n"
        "v_and_b32 v93, 0xffff0000, v134\n"
        "v_pk_mul_f32 v[92:93], v[92:93], s[60:61]\n"
        "v_cvt_pk_bf16_f32 v134, v92, v93\n"
        "v_lshlrev_b32 v94, 16, v135\n"
        "v_and_b32 v95, 0xffff0000, v135\n"
        "v_pk_mul_f32 v[94:95], v[94:95], s[60:61]\n"
        "v_cvt_pk_bf16_f32 v135, v94, v95\n"
        "s_mov_b32 exec_lo, 0\n"
        "v_mov_b32 v134, 0\n"
        "v_mov_b32 v135, 0\n"
        "s_mov_b32 exec_lo, -1\n"
        "ds_write_b128 v100, v[132:135] offset:1024\n"
        "s_waitcnt lgkmcnt(0)\n"
        "s_barrier\n"
        "; ---- m = row maximum over keys 0..31, -m goes into pad dim 28 of Q; then S for block A, tile 0 ----\n"
        ".Lattn_hot_code:\n"
        "ds_read_b64 v[144:145], v22 offset:0\n"
        "ds_read_b64 v[146:147], v22 offset:8\n"
        "ds_read_b64 v[148:149], v22 offset:32\n"
        "ds_read_b64 v[150:151], v23 offset:40\n"
        "ds_read_b128 v[120:123], v101\n"
        "ds_read_b128 v[124:127], v101 offset:1024\n"
        "s_waitcnt lgkmcnt(2)\n"
        "v_mfma_f32_32x32x16_bf16 v[72:87], v[144:147], v[128:131], 0\n"
        "v_mfma_f32_32x32x16_bf16 v[72:87], v[148:151], v[132:135], v[72:87]\n"
        "s_waitcnt lgkmcnt(0)\n"
        "v_mfma_f32_32x32x16_bf16 v[56:71], v[144:147], v[120:123], 0\n"
        "v_mfma_f32_32x32x16_bf16 v[56:71], v[148:151], v[124:127], v[56:71]\n"
        "s_barrier\n"
        "ds_read_b64 v[136:137], v8 offset:0\n"
        "ds_read_b64 v[138:139], v8 offset:8\n"
        "ds_read_b64 v[140:141], v8 offset:32\n"
        "ds_read_b64 v[142:143], v12 offset:40\n"
        "ds_read_b64_tr_b16 v[152:153], v16 offset:0\n"
        "ds_read_b64_tr_b16 v[154:155], v16 offset:224\n"
        "ds_read_b64_tr_b16 v[156:157], v16 offset:896\n"
        "ds_read_b64_tr_b16 v[158:159], v16 offset:1120\n"
        "s_nop 0\n"
        "v_max3_f32 v89, v72, v73, v74\n"
        "v_max3_f32 v91, v75, v76, v77\n"
        "v_max3_f32 v88, v56, v57, v58\n"
        "v_max3_f32 v90, v59, v60, v61\n"
        "v_max3_f32 v88, v88, v62, v63\n"
        "v_max3_f32 v89, v89, v78, v79\n"
        "v_max3_f32 v90, v90, v64, v65\n"
        "v_max3_f32 v91, v91, v80, v81\n"
        "v_max3_f32 v88, v88, v66, v67\n"
        "v_max3_f32 v89, v89, v82, v83\n"
        "v_max3_f32 v90, v90, v68, v69\n"
        "v_max3_f32 v91, v91, v84, v85\n"
        "v_max3_f32 v88, v88, v70, v71\n"
        "v_max3_f32 v89, v89, v86, v87\n"
        "v_max_f32 v88, v88, v90\n"
        "v_max_f32 v89, v89, v91\n"
        "v_mov_b32 v90, v88\n"
        "v_mov_b32 v91, v89\n"
        "s_nop 1\n"
        "v_permlane32_swap_b32 v88, v90\n"
        "v_permlane32_swap_b32 v89, v91\n"
        "s_nop 0\n"
        "v_max_f32 v88, v88, v90\n"
        "v_max_f32 v89, v89, v91\n"
        "s_mov_b32 exec_lo, 0\n"
        "v_cvt_pk_bf16_f32 v126, -v88, 0\n"
        "v_cvt_pk_bf16_f32 v134, -v89, 0\n"
        "s_mov_b32 exec_lo, -1\n"
        "s_waitcnt lgkmcnt(4)\n"
        "s_nop 2\n"
        "v_mfma_f32_32x32x16_bf16 v[56:71], v[136:139], v[120:123], 0\n"
        "v_mfma_f32_32x32x16_bf16 v[56:71], v[140:143], v[124:127], v[56:71]\n"
        "ds_read_b64 v[144:145], v8 offset:1792\n"
        "ds_read_b64 v[146:147], v8 offset:1800\n"
        "ds_read_b64 v[148:149], v8 offset:1824\n"
        "ds_read_b64 v[150:151], v12 offset:1832\n"
        "s_nop 7\n"
        "s_mov_b32 s40, 4\n"
        "; ---- main loop: 4 trips x 4 ring slots x 4 key tiles x (block A, block B) ----\n"
        ".p2align 6\n"
        "1:\n"
        "s_waitcnt lgkmcnt(4)\n"
        "s_setprio 0\n"
        "v_mfma_f32_32x32x16_bf16 v[72:87], v[136:139], v[128:131], 0\n"
        "ds_read_b64_tr_b16 v[160:161], v16 offset:1792\n"
        "v_exp_f32 v56, v56\n"
        "v_exp_f32 v57, v57\n"
        "s_setprio 1\n"
        "v_exp_f32 v58, v58\n"
        "v_exp_f32 v59, v59\n"
        "s_setprio 0\n"
        "v_smfmac_f32_32x32x32_bf16 v[40:55], v[164:167], v[112:119], v20\n"
        "ds_read_b64_tr_b16 v[162:163], v16 offset:2016\n"
        "v_exp_f32 v60, v60\n"
        "v_exp_f32 v61, v61\n"
        "s_setprio 1\n"
        "v_exp_f32 v62, v62\n"
        "v_exp_f32 v63, v63\n"
        "s_setprio 0\n"
        "v_mfma_f32_32x32x16_bf16 v[72:87], v[140:143], v[132:135], v[72:87]\n"
        "ds_read_b64_tr_b16 v[164:165], v16 offset:2688\n"
        "v_exp_f32 v64, v64\n"
        "v_exp_f32 v65, v65\n"
        "s_setprio 1\n"
        "v_exp_f32 v66, v66\n"
        "v_exp_f32 v67, v67\n"
        "s_setprio 0\n"
        "v_smfmac_f32_32x32x32_bf16 v[24:39], v[152:155], v[56:63], v20\n"
        "ds_read_b64_tr_b16 v[166:167], v16 offset:2912\n"
        "v_exp_f32 v68, v68\n"
        "v_exp_f32 v69, v69\n"
        "s_setprio 1\n"
        "v_exp_f32 v70, v70\n"
        "v_exp_f32 v71, v71\n"
        "s_cmp_lg_u32 s40, 4\n"
        "s_cbranch_scc1 8f\n"
        "s_barrier\n"
        "8:\n"
        "s_waitcnt lgkmcnt(4)\n"
        "s_setprio 0\n"
        "v_mfma_f32_32x32x16_bf16 v[88:103], v[144:147], v[120:123], 0\n"
        "ds_read_b64 v[136:137], v8 offset:3584\n"
        "v_exp_f32 v72, v72\n"
        "v_exp_f32 v73, v73\n"
        "s_setprio 1\n"
        "v_exp_f32 v74, v74\n"
        "v_exp_f32 v75, v75\n"
        "s_setprio 0\n"
        "v_smfmac_f32_32x32x32_bf16 v[24:39], v[156:159], v[64:71], v20\n"
        "ds_read_b64 v[138:139], v8 offset:3592\n"
        "v_exp_f32 v76, v76\n"
        "v_exp_f32 v77, v77\n"
        "s_setprio 1\n"
        "v_exp_f32 v78, v78\n"
        "v_exp_f32 v79, v79\n"
        "s_setprio 0\n"
        "v_mfma_f32_32x32x16_bf16 v[88:103], v[148:151], v[124:127], v[88:103]\n"
        "ds_read_b64 v[140:141], v8 offset:3616\n"
        "v_exp_f32 v80, v80\n"
        "v_exp_f32 v81, v81\n"
        "s_setprio 1\n"
        "v_exp_f32 v82, v82\n"
        "v_exp_f32 v83, v83\n"
        "s_setprio 0\n"
        "v_smfmac_f32_32x32x32_bf16 v[40:55], v[152:155], v[72:79], v20\n"
        "ds_read_b64 v[142:143], v12 offset:3624\n"
        "v_exp_f32 v84, v84\n"
        "v_exp_f32 v85, v85\n"
        "s_setprio 1\n"
        "v_exp_f32 v86, v86\n"
        "v_exp_f32 v87, v87\n"
        "s_waitcnt lgkmcnt(4)\n"
        "s_setprio 0\n"
        "v_mfma_f32_32x32x16_bf16 v[104:119], v[144:147], v[128:131], 0\n"
        "ds_read_b64_tr_b16 v[152:153], v16 offset:3584\n"
        "v_exp_f32 v88, v88\n"
        "v_exp_f32 v89, v89\n"
        "s_setprio 1\n"
        "v_exp_f32 v90, v90\n"
        "v_exp_f32 v91, v91\n"
        "s_setprio 0\n"
        "v_smfmac_f32_32x32x32_bf16 v[40:55], v[156:159], v[80:87], v20\n"
        "ds_read_b64_tr_b16 v[154:155], v16 offset:3808\n"
        "v_exp_f32 v92, v92\n"
        "v_exp_f32 v93, v93\n"
        "s_setprio 1\n"
        "v_exp_f32 v94, v94\n"
        "v_exp_f32 v95, v95\n"
        "s_setprio 0\n"
        "v_mfma_f32_32x32x16_bf16 v[104:119], v[148:151], v[132:135], v[104:119]\n"
        "ds_read_b64_tr_b16 v[156:157], v16 offset:4480\n"
        "v_exp_f32 v96, v96\n"
        "v_exp_f32 v97, v97\n"
        "s_setprio 1\n"
        "v_exp_f32 v98, v98\n"
        "v_exp_f32 v99, v99\n"
        "s_setprio 0\n"
        "v_smfmac_f32_32x32x32_bf16 v[24:39], v[160:163], v[88:95], v20\n"
        "ds_read_b64_tr_b16 v[158:159], v16 offset:4704\n"
        "v_exp_f32 v100, v100\n"
        "v_exp_f32 v101, v101\n"
        "s_setprio 1\n"
        "v_exp_f32 v102, v102\n"
        "v_exp_f32 v103, v103\n"
        "s_waitcnt lgkmcnt(4)\n"
        "s_setprio 0\n"
        "v_mfma_f32_32x32x16_bf16 v[56:71], v[136:139], v[120:123], 0\n"
        "ds_read_b64 v[144:145], v8 offset:5376\n"
        "v_exp_f32 v104, v104\n"
        "v_exp_f32 v105, v105\n"
        "s_setprio 1\n"
        "v_exp_f32 v106, v106\n"
        "v_exp_f32 v107, v107\n"
        "s_setprio 0\n"
        "v_smfmac_f32_32x32x32_bf16 v[24:39], v[164:167], v[96:103], v20\n"
        "ds_read_b64 v[146:147], v8 offset:5384\n"
        "v_exp_f32 v108, v108\n"
        "v_exp_f32 v109, v109\n"
        "s_setprio 1\n"
        "v_exp_f32 v110, v110\n"
        "v_exp_f32 v111, v111\n"
        "s_setprio 0\n"
        "v_mfma_f32_32x32x16_bf16 v[56:71], v[140:143], v[124:127], v[56:71]\n"
        "ds_read_b64 v[148:149], v8 offset:5408\n"
        "v_exp_f32 v112, v112\n"
        "v_exp_f32 v113, v113\n"
        "s_setprio 1\n"
        "v_exp_f32 v114, v114\n"
        "v_exp_f32 v115, v115\n"
        "s_setprio 0\n"
        "v_smfmac_f32_32x32x32_bf16 v[40:55], v[160:163], v[104:111], v20\n"
        "ds_read_b64 v[150:151], v12 offset:5416\n"
        "v_exp_f32 v116, v116\n"
        "v_exp_f32 v117, v117\n"
        "s_setprio 1\n"
        "v_exp_f32 v118, v118\n"
        "v_exp_f32 v119, v119\n"
        "s_waitcnt lgkmcnt(4)\n"
        "s_setprio 0\n"
        "v_mfma_f32_32x32x16_bf16 v[72:87], v[136:139], v[128:131], 0\n"
        "ds_read_b64_tr_b16 v[160:161], v16 offset:5376\n"
        "v_exp_f32 v56, v56\n"
        "v_exp_f32 v57, v57\n"
        "s_setprio 1\n"
        "v_exp_f32 v58, v58\n"
        "v_exp_f32 v59, v59\n"
        "s_setprio 0\n"
        "v_smfmac_f32_32x32x32_bf16 v[40:55], v[164:167], v[112:119], v20\n"
        "ds_read_b64_tr_b16 v[162:163], v16 offset:5600\n"
        "v_exp_f32 v60, v60\n"
        "v_exp_f32 v61, v61\n"
        "s_setprio 1\n"
        "v_exp_f32 v62, v62\n"
        "v_exp_f32 v63, v63\n"
        "s_setprio 0\n"
        "v_mfma_f32_32x32x16_bf16 v[72:87], v[140:143], v[132:135], v[72:87]\n"
        "ds_read_b64_tr_b16 v[164:165], v16 offset:6272\n"
        "v_exp_f32 v64, v64\n"
        "v_exp_f32 v65, v65\n"
        "s_setprio 1\n"
        "v_exp_f32 v66, v66\n"
        "v_exp_f32 v67, v67\n"
        "s_setprio 0\n"
        "v_smfmac_f32_32x32x32_bf16 v[24:39], v[152:155], v[56:63], v20\n"
        "ds_read_b64_tr_b16 v[166:167], v16 offset:6496\n"
        "v_exp_f32 v68, v68\n"
        "v_exp_f32 v69, v69\n"
        "s_setprio 1\n"
        "v_exp_f32 v70, v70\n"
        "v_exp_f32 v71, v71\n"
        "s_barrier\n"
        "s_waitcnt lgkmcnt(4)\n"
        "s_setprio 0\n"
        "v_mfma_f32_32x32x16_bf16 v[88:103], v[144:147], v[120:123], 0\n"
        "ds_read_b64 v[136:137], v9 offset:0\n"
        "v_exp_f32 v72, v72\n"
        "v_exp_f32 v73, v73\n"
        "s_setprio 1\n"
        "v_exp_f32 v74, v74\n"
        "v_exp_f32 v75, v75\n"
        "s_setprio 0\n"
        "v_smfmac_f32_32x32x32_bf16 v[24:39], v[156:159], v[64:71], v20\n"
        "ds_read_b64 v[138:139], v9 offset:8\n"
        "v_exp_f32 v76, v76\n"
        "v_exp_f32 v77, v77\n"
        "s_setprio 1\n"
        "v_exp_f32 v78, v78\n"
        "v_exp_f32 v79, v79\n"
        "s_setprio 0\n"
        "v_mfma_f32_32x32x16_bf16 v[88:103], v[148:151], v[124:127], v[88:103]\n"
        "ds_read_b64 v[140:141], v9 offset:32\n"
        "v_exp_f32 v80, v80\n"
        "v_exp_f32 v81, v81\n"
        "s_setprio 1\n"
        "v_exp_f32 v82, v82\n"
        "v_exp_f32 v83, v83\n"
        "s_setprio 0\n"
        "v_smfmac_f32_32x32x32_bf16 v[40:55], v[152:155], v[72:79], v20\n"
        "ds_read_b64 v[142:143], v13 offset:40\n"
        "v_exp_f32 v84, v84\n"
        "v_exp_f32 v85, v85\n"
        "s_setprio 1\n"
        "v_exp_f32 v86, v86\n"
        "v_exp_f32 v87, v87\n"
        "s_waitcnt lgkmcnt(4)\n"
        "s_setprio 0\n"
        "v_mfma_f32_32x32x16_bf16 v[104:119], v[144:147], v[128:131], 0\n"
        "ds_read_b64_tr_b16 v[152:153], v17 offset:0\n"
        "v_exp_f32 v88, v88\n"
        "v_exp_f32 v89, v89\n"
        "s_setprio 1\n"
        "v_exp_f32 v90, v90\n"
        "v_exp_f32 v91, v91\n"
        "s_setprio 0\n"
        "v_smfmac_f32_32x32x32_bf16 v[40:55], v[156:159], v[80:87], v20\n"
        "ds_read_b64_tr_b16 v[154:155], v17 offset:224\n"
        "v_exp_f32 v92, v92\n"
        "v_exp_f32 v93, v93\n"
        "s_setprio 1\n"
        "v_exp_f32 v94, v94\n"
        "v_exp_f32 v95, v95\n"
        "s_setprio 0\n"
        "v_mfma_f32_32x32x16_bf16 v[104:119], v[148:151], v[132:135], v[104:119]\n"
        "ds_read_b64_tr_b16 v[156:157], v17 offset:896\n"
        "v_exp_f32 v96, v96\n"
        "v_exp_f32 v97, v97\n"
        "s_setprio 1\n"
        "v_exp_f32 v98, v98\n"
        "v_exp_f32 v99, v99\n"
        "s_setprio 0\n"
        "v_smfmac_f32_32x32x32_bf16 v[24:39], v[160:163], v[88:95], v20\n"
        "ds_read_b64_tr_b16 v[158:159], v17 offset:1120\n"
        "v_exp_f32 v100, v100\n"
        "v_exp_f32 v101, v101\n"
        "s_setprio 1\n"
        "v_exp_f32 v102, v102\n"
        "v_exp_f32 v103, v103\n"
        "s_waitcnt lgkmcnt(4)\n"
        "s_setprio 0\n"
        "v_mfma_f32_32x32x16_bf16 v[56:71], v[136:139], v[120:123], 0\n"
        "ds_read_b64 v[144:145], v9 offset:1792\n"
        "v_exp_f32 v104, v104\n"
        "v_exp_f32 v105, v105\n"
        "s_setprio 1\n"
        "v_exp_f32 v106, v106\n"
        "v_exp_f32 v107, v107\n"
        "s_setprio 0\n"
        "v_smfmac_f32_32x32x32_bf16 v[24:39], v[164:167], v[96:103], v20\n"
        "ds_read_b64 v[146:147], v9 offset:1800\n"
        "v_exp_f32 v108, v108\n"
        "v_exp_f32 v109, v109\n"
        "s_setprio 1\n"
        "v_exp_f32 v110, v110\n"
        "v_exp_f32 v111, v111\n"
        "s_setprio 0\n"
        "v_mfma_f32_32x32x16_bf16 v[56:71], v[140:143], v[124:127], v[56:71]\n"
        "ds_read_b64 v[148:149], v9 offset:1824\n"
        "v_exp_f32 v112, v112\n"
        "v_exp_f32 v113, v113\n"
        "s_setprio 1\n"
        "v_exp_f32 v114, v114\n"
        "v_exp_f32 v115, v115\n"
        "s_setprio 0\n"
        "v_smfmac_f32_32x32x32_bf16 v[40:55], v[160:163], v[104:111], v20\n"
        "ds_read_b64 v[150:151], v13 offset:1832\n"
        "v_exp_f32 v116, v116\n"
        "v_exp_f32 v117, v117\n"
        "s_setprio 1\n"
        "v_exp_f32 v118, v118\n"
        "v_exp_f32 v119, v119\n"
        "s_waitcnt lgkmcnt(4)\n"
        "s_setprio 0\n"
        "v_mfma_f32_32x32x16_bf16 v[72:87], v[136:139], v[128:131], 0\n"
        "ds_read_b64_tr_b16 v[160:161], v17 offset:1792\n"
        "v_exp_f32 v56, v56\n"
        "v_exp_f32 v57, v57\n"
        "s_setprio 1\n"
        "v_exp_f32 v58, v58\n"
        "v_exp_f32 v59, v59\n"
        "s_setprio 0\n"
        "v_smfmac_f32_32x32x32_bf16 v[40:55], v[164:167], v[112:119], v20\n"
        "ds_read_b64_tr_b16 v[162:163], v17 offset:2016\n"
        "v_exp_f32 v60, v60\n"
        "v_exp_f32 v61, v61\n"
        "s_setprio 1\n"
        "v_exp_f32 v62, v62\n"
        "v_exp_f32 v63, v63\n"
        "s_setprio 0\n"
        "v_mfma_f32_32x32x16_bf16 v[72:87], v[140:143], v[132:135], v[72:87]\n"
        "ds_read_b64_tr_b16 v[164:165], v17 offset:2688\n"
        "v_exp_f32 v64, v64\n"
        "v_exp_f32 v65, v65\n"
        "s_setprio 1\n"
        "v_exp_f32 v66, v66\n"
        "v_exp_f32 v67, v67\n"
        "s_setprio 0\n"
        "v_smfmac_f32_32x32x32_bf16 v[24:39], v[152:155], v[56:63], v20\n"
        "ds_read_b64_tr_b16 v[166:167], v17 offset:2912\n"
        "v_exp_f32 v68, v68\n"
        "v_exp_f32 v69, v69\n"
        "s_setprio 1\n"
        "v_exp_f32 v70, v70\n"
        "v_exp_f32 v71, v71\n"
        "s_waitcnt lgkmcnt(4)\n"
        "s_setprio 0\n"
        "v_mfma_f32_32x32x16_bf16 v[88:103], v[144:147], v[120:123], 0\n"
        "ds_read_b64 v[136:137], v9 offset:3584\n"
        "v_exp_f32 v72, v72\n"
        "v_exp_f32 v73, v73\n"
        "s_setprio 1\n"
        "v_exp_f32 v74, v74\n"
        "v_exp_f32 v75, v75\n"
        "s_setprio 0\n"
        "v_smfmac_f32_32x32x32_bf16 v[24:39], v[156:159], v[64:71], v20\n"
        "ds_read_b64 v[138:139], v9 offset:3592\n"
        "v_exp_f32 v76, v76\n"
        "v_exp_f32 v77, v77\n"
        "s_setprio 1\n"
        "v_exp_f32 v78, v78\n"
        "v_exp_f32 v79, v79\n"
        "s_setprio 0\n"
        "v_mfma_f32_32x32x16_bf16 v[88:103], v[148:151], v[124:127], v[88:103]\n"
        "ds_read_b64 v[140:141], v9 offset:3616\n"
        "v_exp_f32 v80, v80\n"
        "v_exp_f32 v81, v81\n"
        "s_setprio 1\n"
        "v_exp_f32 v82, v82\n"
        "v_exp_f32 v83, v83\n"
        "s_setprio 0\n"
        "v_smfmac_f32_32x32x32_bf16 v[40:55], v[152:155], v[72:79], v20\n"
        "ds_read_b64 v[142:143], v13 offset:3624\n"
        "v_exp_f32 v84, v84\n"
        "v_exp_f32 v85, v85\n"
        "s_setprio 1\n"
        "v_exp_f32 v86, v86\n"
        "v_exp_f32 v87, v87\n"
        "s_waitcnt lgkmcnt(4)\n"
        "s_setprio 0\n"
        "v_mfma_f32_32x32x16_bf16 v[104:119], v[144:147], v[128:131], 0\n"
        "ds_read_b64_tr_b16 v[152:153], v17 offset:3584\n"
        "v_exp_f32 v88, v88\n"
        "v_exp_f32 v89, v89\n"
        "s_setprio 1\n"
        "v_exp_f32 v90, v90\n"
        "v_exp_f32 v91, v91\n"
        "s_setprio 0\n"
        "v_smfmac_f32_32x32x32_bf16 v[40:55], v[156:159], v[80:87], v20\n"
        "ds_read_b64_tr_b16 v[154:155], v17 offset:3808\n"
        "v_exp_f32 v92, v92\n"
        "v_exp_f32 v93, v93\n"
        "s_setprio 1\n"
        "v_exp_f32 v94, v94\n"
        "v_exp_f32 v95, v95\n"
        "s_setprio 0\n"
        "v_mfma_f32_32x32x16_bf16 v[104:119], v[148:151], v[132:135], v[104:119]\n"
        "ds_read_b64_tr_b16 v[156:157], v17 offset:4480\n"
        "v_exp_f32 v96, v96\n"
        "v_exp_f32 v97, v97\n"
        "s_setprio 1\n"
        "v_exp_f32 v98, v98\n"
        "v_exp_f32 v99, v99\n"
        "s_setprio 0\n"
        "v_smfmac_f32_32x32x32_bf16 v[24:39], v[160:163], v[88:95], v20\n"
        "ds_read_b64_tr_b16 v[158:159], v17 offset:4704\n"
        "v_exp_f32 v100, v100\n"
        "v_exp_f32 v101, v101\n"
        "s_setprio 1\n"
        "v_exp_f32 v102, v102\n"
        "v_exp_f32 v103, v103\n"
        "s_waitcnt lgkmcnt(4)\n"
        "s_setprio 0\n"
        "v_mfma_f32_32x32x16_bf16 v[56:71], v[136:139], v[120:123], 0\n"
        "ds_read_b64 v[144:145], v9 offset:5376\n"
        "v_exp_f32 v104, v104\n"
        "v_exp_f32 v105, v105\n"
        "s_setprio 1\n"
        "v_exp_f32 v106, v106\n"
        "v_exp_f32 v107, v107\n"
        "s_setprio 0\n"
        "v_smfmac_f32_32x32x32_bf16 v[24:39], v[164:167], v[96:103], v20\n"
        "ds_read_b64 v[146:147], v9 offset:5384\n"
        "v_exp_f32 v108, v108\n"
        "v_exp_f32 v109, v109\n"
        "s_setprio 1\n"
        "v_exp_f32 v110, v110\n"
        "v_exp_f32 v111, v111\n"
        "s_setprio 0\n"
        "v_mfma_f32_32x32x16_bf16 v[56:71], v[140:143], v[124:127], v[56:71]\n"
        "ds_read_b64 v[148:149], v9 offset:5408\n"
        "v_exp_f32 v112, v112\n"
        "v_exp_f32 v113, v113\n"
        "s_setprio 1\n"
        "v_exp_f32 v114, v114\n"
        "v_exp_f32 v115, v115\n"
        "s_setprio 0\n"
        "v_smfmac_f32_32x32x32_bf16 v[40:55], v[160:163], v[104:111], v20\n"
        "ds_read_b64 v[150:151], v13 offset:5416\n"
        "v_exp_f32 v116, v116\n"
        "v_exp_f32 v117, v117\n"
        "s_setprio 1\n"
        "v_exp_f32 v118, v118\n"
        "v_exp_f32 v119, v119\n"
        "s_waitcnt lgkmcnt(4)\n"
        "s_setprio 0\n"
        "v_mfma_f32_32x32x16_bf16 v[72:87], v[136:139], v[128:131], 0\n"
        "ds_read_b64_tr_b16 v[160:161], v17 offset:5376\n"
        "v_exp_f32 v56, v56\n"
        "v_exp_f32 v57, v57\n"
        "s_setprio 1\n"
        "v_exp_f32 v58, v58\n"
        "v_exp_f32 v59, v59\n"
        "s_setprio 0\n"
        "v_smfmac_f32_32x32x32_bf16 v[40:55], v[164:167], v[112:119], v20\n"
        "ds_read_b64_tr_b16 v[162:163], v17 offset:5600\n"
        "v_exp_f32 v60, v60\n"
        "v_exp_f32 v61, v61\n"
        "s_setprio 1\n"
        "v_exp_f32 v62, v62\n"
        "v_exp_f32 v63, v63\n"
        "s_setprio 0\n"
        "v_mfma_f32_32x32x16_bf16 v[72:87], v[140:143], v[132:135], v[72:87]\n"
        "ds_read_b64_tr_b16 v[164:165], v17 offset:6272\n"
        "v_exp_f32 v64, v64\n"
        "v_exp_f32 v65, v65\n"
        "s_setprio 1\n"
        "v_exp_f32 v66, v66\n"
        "v_exp_f32 v67, v67\n"
        "s_setprio 0\n"
        "v_smfmac_f32_32x32x32_bf16 v[24:39], v[152:155], v[56:63], v20\n"
        "ds_read_b64_tr_b16 v[166:167], v17 offset:6496\n"
        "v_exp_f32 v68, v68\n"
        "v_exp_f32 v69, v69\n"
        "s_setprio 1\n"
        "v_exp_f32 v70, v70\n"
        "v_exp_f32 v71, v71\n"
        "s_barrier\n"
        "s_waitcnt lgkmcnt(4)\n"
        "s_setprio 0\n"
        "v_mfma_f32_32x32x16_bf16 v[88:103], v[144:147], v[120:123], 0\n"
        "ds_read_b64 v[136:137], v10 offset:0\n"
        "v_exp_f32 v72, v72\n"
        "v_exp_f32 v73, v73\n"
        "s_setprio 1\n"
        "v_exp_f32 v74, v74\n"
        "v_exp_f32 v75, v75\n"
        "s_setprio 0\n"
        "v_smfmac_f32_32x32x32_bf16 v[24:39], v[156:159], v[64:71], v20\n"
        "ds_read_b64 v[138:139], v10 offset:8\n"
        "v_exp_f32 v76, v76\n"
        "v_exp_f32 v77, v77\n"
        "s_setprio 1\n"
        "v_exp_f32 v78, v78\n"
        "v_exp_f32 v79, v79\n"
        "s_setprio 0\n"
        "v_mfma_f32_32x32x16_bf16 v[88:103], v[148:151], v[124:127], v[88:103]\n"
        "ds_read_b64 v[140:141], v10 offset:32\n"
        "v_exp_f32 v80, v80\n"
        "v_exp_f32 v81, v81\n"
        "s_setprio 1\n"
        "v_exp_f32 v82, v82\n"
        "v_exp_f32 v83, v83\n"
        "s_setprio 0\n"
        "v_smfmac_f32_32x32x32_bf16 v[40:55], v[152:155], v[72:79], v20\n"
        "ds_read_b64 v[142:143], v14 offset:40\n"
        "v_exp_f32 v84, v84\n"
        "v_exp_f32 v85, v85\n"
        "s_setprio 1\n"
        "v_exp_f32 v86, v86\n"
        "v_exp_f32 v87, v87\n"
        "s_waitcnt lgkmcnt(4)\n"
        "s_setprio 0\n"
        "v_mfma_f32_32x32x16_bf16 v[104:119], v[144:147], v[128:131], 0\n"
        "ds_read_b64_tr_b16 v[152:153], v18 offset:0\n"
        "v_exp_f32 v88, v88\n"
        "v_exp_f32 v89, v89\n"
        "s_setprio 1\n"
        "v_exp_f32 v90, v90\n"
        "v_exp_f32 v91, v91\n"
        "s_setprio 0\n"
        "v_smfmac_f32_32x32x32_bf16 v[40:55], v[156:159], v[80:87], v20\n"
        "ds_read_b64_tr_b16 v[154:155], v18 offset:224\n"
        "v_exp_f32 v92, v92\n"
        "v_exp_f32 v93, v93\n"
        "s_setprio 1\n"
        "v_exp_f32 v94, v94\n"
        "v_exp_f32 v95, v95\n"
        "s_setprio 0\n"
        "v_mfma_f32_32x32x16_bf16 v[104:119], v[148:151], v[132:135], v[104:119]\n"
        "ds_read_b64_tr_b16 v[156:157], v18 offset:896\n"
        "v_exp_f32 v96, v96\n"
        "v_exp_f32 v97, v97\n"
        "s_setprio 1\n"
        "v_exp_f32 v98, v98\n"
        "v_exp_f32 v99, v99\n"
        "s_setprio 0\n"
        "v_smfmac_f32_32x32x32_bf16 v[24:39], v[160:163], v[88:95], v20\n"
        "ds_read_b64_tr_b16 v[158:159], v18 offset:1120\n"
        "v_exp_f32 v100, v100\n"
        "v_exp_f32 v101, v101\n"
        "s_setprio 1\n"
        "v_exp_f32 v102, v102\n"
        "v_exp_f32 v103, v103\n"
        "s_waitcnt lgkmcnt(4)\n"
        "s_setprio 0\n"
        "v_mfma_f32_32x32x16_bf16 v[56:71], v[136:139], v[120:123], 0\n"
        "ds_read_b64 v[144:145], v10 offset:1792\n"
        "v_exp_f32 v104, v104\n"
        "v_exp_f32 v105, v105\n"
        "s_setprio 1\n"
        "v_exp_f32 v106, v106\n"
        "v_exp_f32 v107, v107\n"
        "s_setprio 0\n"
        "v_smfmac_f32_32x32x32_bf16 v[24:39], v[164:167], v[96:103], v20\n"
        "ds_read_b64 v[146:147], v10 offset:1800\n"
        "v_exp_f32 v108, v108\n"
        "v_exp_f32 v109, v109\n"
        "s_setprio 1\n"
        "v_exp_f32 v110, v110\n"
        "v_exp_f32 v111, v111\n"
        "s_setprio 0\n"
        "v_mfma_f32_32x32x16_bf16 v[56:71], v[140:143], v[124:127], v[56:71]\n"
        "ds_read_b64 v[148:149], v10 offset:1824\n"
        "v_exp_f32 v112, v112\n"
        "v_exp_f32 v113, v113\n"
        "s_setprio 1\n"
        "v_exp_f32 v114, v114\n"
        "v_exp_f32 v115, v115\n"
        "s_setprio 0\n"
        "v_smfmac_f32_32x32x32_bf16 v[40:55], v[160:163], v[104:111], v20\n"
        "ds_read_b64 v[150:151], v14 offset:1832\n"
        "v_exp_f32 v116, v116\n"
        "v_exp_f32 v117, v117\n"
        "s_setprio 1\n"
        "v_exp_f32 v118, v118\n"
        "v_exp_f32 v119, v119\n"
        "s_waitcnt lgkmcnt(4)\n"
        "s_setprio 0\n"
        "v_mfma_f32_32x32x16_bf16 v[72:87], v[136:139], v[128:131], 0\n"
        "ds_read_b64_tr_b16 v[160:161], v18 offset:1792\n"
        "v_exp_f32 v56, v56\n"
        "v_exp_f32 v57, v57\n"
        "s_setprio 1\n"
        "v_exp_f32 v58, v58\n"
        "v_exp_f32 v59, v59\n"
        "s_setprio 0\n"
        "v_smfmac_f32_32x32x32_bf16 v[40:55], v[164:167], v[112:119], v20\n"
        "ds_read_b64_tr_b16 v[162:163], v18 offset:2016\n"
        "v_exp_f32 v60, v60\n"
        "v_exp_f32 v61, v61\n"
        "s_setprio 1\n"
        "v_exp_f32 v62, v62\n"
        "v_exp_f32 v63, v63\n"
        "s_setprio 0\n"
        "v_mfma_f32_32x32x16_bf16 v[72:87], v[140:143], v[132:135], v[72:87]\n"
        "ds_read_b64_tr_b16 v[164:165], v18 offset:2688\n"
        "v_exp_f32 v64, v64\n"
        "v_exp_f32 v65, v65\n"
        "s_setprio 1\n"
        "v_exp_f32 v66, v66\n"
        "v_exp_f32 v67, v67\n"
        "s_setprio 0\n"
        "v_smfmac_f32_32x32x32_bf16 v[24:39], v[152:155], v[56:63], v20\n"
        "ds_read_b64_tr_b16 v[166:167], v18 offset:2912\n"
        "v_exp_f32 v68, v68\n"
        "v_exp_f32 v69, v69\n"
        "s_setprio 1\n"
        "v_exp_f32 v70, v70\n"
        "v_exp_f32 v71, v71\n"
        "s_waitcnt lgkmcnt(4)\n"
        "s_setprio 0\n"
        "v_mfma_f32_32x32x16_bf16 v[88:103], v[144:147], v[120:123], 0\n"
        "ds_read_b64 v[136:137], v10 offset:3584\n"
        "v_exp_f32 v72, v72\n"
        "v_exp_f32 v73, v73\n"
        "s_setprio 1\n"
        "v_exp_f32 v74, v74\n"
        "v_exp_f32 v75, v75\n"
        "s_setprio 0\n"
        "v_smfmac_f32_32x32x32_bf16 v[24:39], v[156:159], v[64:71], v20\n"
        "ds_read_b64 v[138:139], v10 offset:3592\n"
        "v_exp_f32 v76, v76\n"
        "v_exp_f32 v77, v77\n"
        "s_setprio 1\n"
        "v_exp_f32 v78, v78\n"
        "v_exp_f32 v79, v79\n"
        "s_setprio 0\n"
        "v_mfma_f32_32x32x16_bf16 v[88:103], v[148:151], v[124:127], v[88:103]\n"
        "ds_read_b64 v[140:141], v10 offset:3616\n"
        "v_exp_f32 v80, v80\n"
        "v_exp_f32 v81, v81\n"
        "s_setprio 1\n"
        "v_exp_f32 v82, v82\n"
        "v_exp_f32 v83, v83\n"
        "s_setprio 0\n"
        "v_smfmac_f32_32x32x32_bf16 v[40:55], v[152:155], v[72:79], v20\n"
        "ds_read_b64 v[142:143], v14 offset:3624\n"
        "v_exp_f32 v84, v84\n"
        "v_exp_f32 v85, v85\n"
        "s_setprio 1\n"
        "v_exp_f32 v86, v86\n"
        "v_exp_f32 v87, v87\n"
        "s_waitcnt lgkmcnt(4)\n"
        "s_setprio 0\n"
        "v_mfma_f32_32x32x16_bf16 v[104:119], v[144:147], v[128:131], 0\n"
        "ds_read_b64_tr_b16 v[152:153], v18 offset:3584\n"
        "v_exp_f32 v88, v88\n"
        "v_exp_f32 v89, v89\n"
        "s_setprio 1\n"
        "v_exp_f32 v90, v90\n"
        "v_exp_f32 v91, v91\n"
        "s_setprio 0\n"
        "v_smfmac_f32_32x32x32_bf16 v[40:55], v[156:159], v[80:87], v20\n"
        "ds_read_b64_tr_b16 v[154:155], v18 offset:3808\n"
        "v_exp_f32 v92, v92\n"
        "v_exp_f32 v93, v93\n"
        "s_setprio 1\n"
        "v_exp_f32 v94, v94\n"
        "v_exp_f32 v95, v95\n"
        "s_setprio 0\n"
        "v_mfma_f32_32x32x16_bf16 v[104:119], v[148:151], v[132:135], v[104:119]\n"
        "ds_read_b64_tr_b16 v[156:157], v18 offset:4480\n"
        "v_exp_f32 v96, v96\n"
        "v_exp_f32 v97, v97\n"
        "s_setprio 1\n"
        "v_exp_f32 v98, v98\n"
        "v_exp_f32 v99, v99\n"
        "s_setprio 0\n"
        "v_smfmac_f32_32x32x32_bf16 v[24:39], v[160:163], v[88:95], v20\n"
        "ds_read_b64_tr_b16 v[158:159], v18 offset:4704\n"
        "v_exp_f32 v100, v100\n"
        "v_exp_f32 v101, v101\n"
        "s_setprio 1\n"
        "v_exp_f32 v102, v102\n"
        "v_exp_f32 v103, v103\n"
        "s_waitcnt lgkmcnt(4)\n"
        "s_setprio 0\n"
        "v_mfma_f32_32x32x16_bf16 v[56:71], v[136:139], v[120:123], 0\n"
        "ds_read_b64 v[144:145], v10 offset:5376\n"
        "v_exp_f32 v104, v104\n"
        "v_exp_f32 v105, v105\n"
        "s_setprio 1\n"
        "v_exp_f32 v106, v106\n"
        "v_exp_f32 v107, v107\n"
        "s_setprio 0\n"
        "v_smfmac_f32_32x32x32_bf16 v[24:39], v[164:167], v[96:103], v20\n"
        "ds_read_b64 v[146:147], v10 offset:5384\n"
        "v_exp_f32 v108, v108\n"
        "v_exp_f32 v109, v109\n"
        "s_setprio 1\n"
        "v_exp_f32 v110, v110\n"
        "v_exp_f32 v111, v111\n"
        "s_setprio 0\n"
        "v_mfma_f32_32x32x16_bf16 v[56:71], v[140:143], v[124:127], v[56:71]\n"
        "ds_read_b64 v[148:149], v10 offset:5408\n"
        "v_exp_f32 v112, v112\n"
        "v_exp_f32 v113, v113\n"
        "s_setprio 1\n"
        "v_exp_f32 v114, v114\n"
        "v_exp_f32 v115, v115\n"
        "s_setprio 0\n"
        "v_smfmac_f32_32x32x32_bf16 v[40:55], v[160:163], v[104:111], v20\n"
        "ds_read_b64 v[150:151], v14 offset:5416\n"
        "v_exp_f32 v116, v116\n"
        "v_exp_f32 v117, v117\n"
        "s_setprio 1\n"
        "v_exp_f32 v118, v118\n"
        "v_exp_f32 v119, v119\n"
        "s_waitcnt lgkmcnt(4)\n"
        "s_setprio 0\n"
        "v_mfma_f32_32x32x16_bf16 v[72:87], v[136:139], v[128:131], 0\n"
        "ds_read_b64_tr_b16 v[160:161], v18 offset:5376\n"
        "v_exp_f32 v56, v56\n"
        "v_exp_f32 v57, v57\n"
        "s_setprio 1\n"
        "v_exp_f32 v58, v58\n"
        "v_exp_f32 v59, v59\n"
        "s_setprio 0\n"
        "v_smfmac_f32_32x32x32_bf16 v[40:55], v[164:167], v[112:119], v20\n"
        "ds_read_b64_tr_b16 v[162:163], v18 offset:5600\n"
        "v_exp_f32 v60, v60\n"
        "v_exp_f32 v61, v61\n"
        "s_setprio 1\n"
        "v_exp_f32 v62, v62\n"
        "v_exp_f32 v63, v63\n"
        "s_setprio 0\n"
        "v_mfma_f32_32x32x16_bf16 v[72:87], v[140:143], v[132:135], v[72:87]\n"
        "ds_read_b64_tr_b16 v[164:165], v18 offset:6272\n"
        "v_exp_f32 v64, v64\n"
        "v_exp_f32 v65, v65\n"
        "s_setprio 1\n"
        "v_exp_f32 v66, v66\n"
        "v_exp_f32 v67, v67\n"
        "s_setprio 0\n"
        "v_smfmac_f32_32x32x32_bf16 v[24:39], v[152:155], v[56:63], v20\n"
        "ds_read_b64_tr_b16 v[166:167], v18 offset:6496\n"
        "v_exp_f32 v68, v68\n"
        "v_exp_f32 v69, v69\n"
        "s_setprio 1\n"
        "v_exp_f32 v70, v70\n"
        "v_exp_f32 v71, v71\n"
        "s_barrier\n"
        "s_waitcnt lgkmcnt(4)\n"
        "s_setprio 0\n"
        "v_mfma_f32_32x32x16_bf16 v[88:103], v[144:147], v[120:123], 0\n"
        "ds_read_b64 v[136:137], v11 offset:0\n"
        "v_exp_f32 v72, v72\n"
        "v_exp_f32 v73, v73\n"
        "s_setprio 1\n"
        "v_exp_f32 v74, v74\n"
        "v_exp_f32 v75, v75\n"
        "s_setprio 0\n"
        "v_smfmac_f32_32x32x32_bf16 v[24:39], v[156:159], v[64:71], v20\n"
        "ds_read_b64 v[138:139], v11 offset:8\n"
        "v_exp_f32 v76, v76\n"
        "v_exp_f32 v77, v77\n"
        "s_setprio 1\n"
        "v_exp_f32 v78, v78\n"
        "v_exp_f32 v79, v79\n"
        "s_setprio 0\n"
        "v_mfma_f32_32x32x16_bf16 v[88:103], v[148:151], v[124:127], v[88:103]\n"
        "ds_read_b64 v[140:141], v11 offset:32\n"
        "v_exp_f32 v80, v80\n"
        "v_exp_f32 v81, v81\n"
        "s_setprio 1\n"
        "v_exp_f32 v82, v82\n"
        "v_exp_f32 v83, v83\n"
        "s_setprio 0\n"
        "v_smfmac_f32_32x32x32_bf16 v[40:55], v[152:155], v[72:79], v20\n"
        "ds_read_b64 v[142:143], v15 offset:40\n"
        "v_exp_f32 v84, v84\n"
        "v_exp_f32 v85, v85\n"
        "s_setprio 1\n"
        "v_exp_f32 v86, v86\n"
        "v_exp_f32 v87, v87\n"
        "s_waitcnt lgkmcnt(4)\n"
        "s_setprio 0\n"
        "v_mfma_f32_32x32x16_bf16 v[104:119], v[144:147], v[128:131], 0\n"
        "ds_read_b64_tr_b16 v[152:153], v19 offset:0\n"
        "v_exp_f32 v88, v88\n"
        "v_exp_f32 v89, v89\n"
        "s_setprio 1\n"
        "v_exp_f32 v90, v90\n"
        "v_exp_f32 v91, v91\n"
        "s_setprio 0\n"
        "v_smfmac_f32_32x32x32_bf16 v[40:55], v[156:159], v[80:87], v20\n"
        "ds_read_b64_tr_b16 v[154:155], v19 offset:224\n"
        "v_exp_f32 v92, v92\n"
        "v_exp_f32 v93, v93\n"
        "s_setprio 1\n"
        "v_exp_f32 v94, v94\n"
        "v_exp_f32 v95, v95\n"
        "s_setprio 0\n"
        "v_mfma_f32_32x32x16_bf16 v[104:119], v[148:151], v[132:135], v[104:119]\n"
        "ds_read_b64_tr_b16 v[156:157], v19 offset:896\n"
        "v_exp_f32 v96, v96\n"
        "v_exp_f32 v97, v97\n"
        "s_setprio 1\n"
        "v_exp_f32 v98, v98\n"
        "v_exp_f32 v99, v99\n"
        "s_setprio 0\n"
        "v_smfmac_f32_32x32x32_bf16 v[24:39], v[160:163], v[88:95], v20\n"
        "ds_read_b64_tr_b16 v[158:159], v19 offset:1120\n"
        "v_exp_f32 v100, v100\n"
        "v_exp_f32 v101, v101\n"
        "s_setprio 1\n"
        "v_exp_f32 v102, v102\n"
        "v_exp_f32 v103, v103\n"
        "s_waitcnt lgkmcnt(4)\n"
        "s_setprio 0\n"
        "v_mfma_f32_32x32x16_bf16 v[56:71], v[136:139], v[120:123], 0\n"
        "ds_read_b64 v[144:145], v11 offset:1792\n"
        "v_exp_f32 v104, v104\n"
        "v_exp_f32 v105, v105\n"
        "s_setprio 1\n"
        "v_exp_f32 v106, v106\n"
        "v_exp_f32 v107, v107\n"
        "s_setprio 0\n"
        "v_smfmac_f32_32x32x32_bf16 v[24:39], v[164:167], v[96:103], v20\n"
        "ds_read_b64 v[146:147], v11 offset:1800\n"
        "v_exp_f32 v108, v108\n"
        "v_exp_f32 v109, v109\n"
        "s_setprio 1\n"
        "v_exp_f32 v110, v110\n"
        "v_exp_f32 v111, v111\n"
        "s_setprio 0\n"
        "v_mfma_f32_32x32x16_bf16 v[56:71], v[140:143], v[124:127], v[56:71]\n"
        "ds_read_b64 v[148:149], v11 offset:1824\n"
        "v_exp_f32 v112, v112\n"
        "v_exp_f32 v113, v113\n"
        "s_setprio 1\n"
        "v_exp_f32 v114, v114\n"
        "v_exp_f32 v115, v115\n"
        "s_setprio 0\n"
        "v_smfmac_f32_32x32x32_bf16 v[40:55], v[160:163], v[104:111], v20\n"
        "ds_read_b64 v[150:151], v15 offset:1832\n"
        "v_exp_f32 v116, v116\n"
        "v_exp_f32 v117, v117\n"
        "s_setprio 1\n"
        "v_exp_f32 v118, v118\n"
        "v_exp_f32 v119, v119\n"
        "s_waitcnt lgkmcnt(4)\n"
        "s_setprio 0\n"
        "v_mfma_f32_32x32x16_bf16 v[72:87], v[136:139], v[128:131], 0\n"
        "ds_read_b64_tr_b16 v[160:161], v19 offset:1792\n"
        "v_exp_f32 v56, v56\n"
        "v_exp_f32 v57, v57\n"
        "s_setprio 1\n"
        "v_exp_f32 v58, v58\n"
        "v_exp_f32 v59, v59\n"
        "s_setprio 0\n"
        "v_smfmac_f32_32x32x32_bf16 v[40:55], v[164:167], v[112:119], v20\n"
        "ds_read_b64_tr_b16 v[162:163], v19 offset:2016\n"
        "v_exp_f32 v60, v60\n"
        "v_exp_f32 v61, v61\n"
        "s_setprio 1\n"
        "v_exp_f32 v62, v62\n"
        "v_exp_f32 v63, v63\n"
        "s_setprio 0\n"
        "v_mfma_f32_32x32x16_bf16 v[72:87], v[140:143], v[132:135], v[72:87]\n"
        "ds_read_b64_tr_b16 v[164:165], v19 offset:2688\n"
        "v_exp_f32 v64, v64\n"
        "v_exp_f32 v65, v65\n"
        "s_setprio 1\n"
        "v_exp_f32 v66, v66\n"
        "v_exp_f32 v67, v67\n"
        "s_setprio 0\n"
        "v_smfmac_f32_32x32x32_bf16 v[24:39], v[152:155], v[56:63], v20\n"
        "ds_read_b64_tr_b16 v[166:167], v19 offset:2912\n"
        "v_exp_f32 v68, v68\n"
        "v_exp_f32 v69, v69\n"
        "s_setprio 1\n"
        "v_exp_f32 v70, v70\n"
        "v_exp_f32 v71, v71\n"
        "s_waitcnt lgkmcnt(4)\n"
        "s_setprio 0\n"
        "v_mfma_f32_32x32x16_bf16 v[88:103], v[144:147], v[120:123], 0\n"
        "ds_read_b64 v[136:137], v11 offset:3584\n"
        "v_exp_f32 v72, v72\n"
        "v_exp_f32 v73, v73\n"
        "s_setprio 1\n"
        "v_exp_f32 v74, v74\n"
        "v_exp_f32 v75, v75\n"
        "s_setprio 0\n"
        "v_smfmac_f32_32x32x32_bf16 v[24:39], v[156:159], v[64:71], v20\n"
        "ds_read_b64 v[138:139], v11 offset:3592\n"
        "v_exp_f32 v76, v76\n"
        "v_exp_f32 v77, v77\n"
        "s_setprio 1\n"
        "v_exp_f32 v78, v78\n"
        "v_exp_f32 v79, v79\n"
        "s_setprio 0\n"
        "v_mfma_f32_32x32x16_bf16 v[88:103], v[148:151], v[124:127], v[88:103]\n"
        "ds_read_b64 v[140:141], v11 offset:3616\n"
        "v_exp_f32 v80, v80\n"
        "v_exp_f32 v81, v81\n"
        "s_setprio 1\n"
        "v_exp_f32 v82, v82\n"
        "v_exp_f32 v83, v83\n"
        "s_setprio 0\n"
        "v_smfmac_f32_32x32x32_bf16 v[40:55], v[152:155], v[72:79], v20\n"
        "ds_read_b64 v[142:143], v15 offset:3624\n"
        "v_exp_f32 v84, v84\n"
        "v_exp_f32 v85, v85\n"
        "s_setprio 1\n"
        "v_exp_f32 v86, v86\n"
        "v_exp_f32 v87, v87\n"
        "s_waitcnt lgkmcnt(4)\n"
        "s_setprio 0\n"
        "v_mfma_f32_32x32x16_bf16 v[104:119], v[144:147], v[128:131], 0\n"
        "ds_read_b64_tr_b16 v[152:153], v19 offset:3584\n"
        "v_exp_f32 v88, v88\n"
        "v_exp_f32 v89, v89\n"
        "s_setprio 1\n"
        "v_exp_f32 v90, v90\n"
        "v_exp_f32 v91, v91\n"
        "s_setprio 0\n"
        "v_smfmac_f32_32x32x32_bf16 v[40:55], v[156:159], v[80:87], v20\n"
        "ds_read_b64_tr_b16 v[154:155], v19 offset:3808\n"
        "v_exp_f32 v92, v92\n"
        "v_exp_f32 v93, v93\n"
        "s_setprio 1\n"
        "v_exp_f32 v94, v94\n"
        "v_exp_f32 v95, v95\n"
        "s_setprio 0\n"
        "v_mfma_f32_32x32x16_bf16 v[104:119], v[148:151], v[132:135], v[104:119]\n"
        "ds_read_b64_tr_b16 v[156:157], v19 offset:4480\n"
        "v_exp_f32 v96, v96\n"
        "v_exp_f32 v97, v97\n"
        "s_setprio 1\n"
        "v_exp_f32 v98, v98\n"
        "v_exp_f32 v99, v99\n"
        "s_setprio 0\n"
        "v_smfmac_f32_32x32x32_bf16 v[24:39], v[160:163], v[88:95], v20\n"
        "ds_read_b64_tr_b16 v[158:159], v19 offset:4704\n"
        "v_exp_f32 v100, v100\n"
        "v_exp_f32 v101, v101\n"
        "s_setprio 1\n"
        "v_exp_f32 v102, v102\n"
        "v_exp_f32 v103, v103\n"
        "s_waitcnt lgkmcnt(4)\n"
        "s_setprio 0\n"
        "v_mfma_f32_32x32x16_bf16 v[56:71], v[136:139], v[120:123], 0\n"
        "ds_read_b64 v[144:145], v11 offset:5376\n"
        "v_exp_f32 v104, v104\n"
        "v_exp_f32 v105, v105\n"
        "s_setprio 1\n"
        "v_exp_f32 v106, v106\n"
        "v_exp_f32 v107, v107\n"
        "s_setprio 0\n"
        "v_smfmac_f32_32x32x32_bf16 v[24:39], v[164:167], v[96:103], v20\n"
        "ds_read_b64 v[146:147], v11 offset:5384\n"
        "v_exp_f32 v108, v108\n"
        "v_exp_f32 v109, v109\n"
        "s_setprio 1\n"
        "v_exp_f32 v110, v110\n"
        "v_exp_f32 v111, v111\n"
        "s_setprio 0\n"
        "v_mfma_f32_32x32x16_bf16 v[56:71], v[140:143], v[124:127], v[56:71]\n"
        "ds_read_b64 v[148:149], v11 offset:5408\n"
        "v_exp_f32 v112, v112\n"
        "v_exp_f32 v113, v113\n"
        "s_setprio 1\n"
        "v_exp_f32 v114, v114\n"
        "v_exp_f32 v115, v115\n"
        "s_setprio 0\n"
        "v_smfmac_f32_32x32x32_bf16 v[40:55], v[160:163], v[104:111], v20\n"
        "ds_read_b64 v[150:151], v15 offset:5416\n"
        "v_exp_f32 v116, v116\n"
        "v_exp_f32 v117, v117\n"
        "s_setprio 1\n"
        "v_exp_f32 v118, v118\n"
        "v_exp_f32 v119, v119\n"
        "s_waitcnt lgkmcnt(4)\n"
        "s_setprio 0\n"
        "v_mfma_f32_32x32x16_bf16 v[72:87], v[136:139], v[128:131], 0\n"
        "ds_read_b64_tr_b16 v[160:161], v19 offset:5376\n"
        "v_exp_f32 v56, v56\n"
        "v_exp_f32 v57, v57\n"
        "s_setprio 1\n"
        "v_exp_f32 v58, v58\n"
        "v_exp_f32 v59, v59\n"
        "s_setprio 0\n"
        "v_smfmac_f32_32x32x32_bf16 v[40:55], v[164:167], v[112:119], v20\n"
        "ds_read_b64_tr_b16 v[162:163], v19 offset:5600\n"
        "v_exp_f32 v60, v60\n"
        "v_exp_f32 v61, v61\n"
        "s_setprio 1\n"
        "v_exp_f32 v62, v62\n"
        "v_exp_f32 v63, v63\n"
        "s_setprio 0\n"
        "v_mfma_f32_32x32x16_bf16 v[72:87], v[140:143], v[132:135], v[72:87]\n"
        "ds_read_b64_tr_b16 v[164:165], v19 offset:6272\n"
        "v_exp_f32 v64, v64\n"
        "v_exp_f32 v65, v65\n"
        "s_setprio 1\n"
        "v_exp_f32 v66, v66\n"
        "v_exp_f32 v67, v67\n"
        "s_setprio 0\n"
        "v_smfmac_f32_32x32x32_bf16 v[24:39], v[152:155], v[56:63], v20\n"
        "ds_read_b64_tr_b16 v[166:167], v19 offset:6496\n"
        "v_exp_f32 v68, v68\n"
        "v_exp_f32 v69, v69\n"
        "s_setprio 1\n"
        "v_exp_f32 v70, v70\n"
        "v_exp_f32 v71, v71\n"
        "s_barrier\n"
        "s_waitcnt lgkmcnt(4)\n"
        "s_setprio 0\n"
        "v_mfma_f32_32x32x16_bf16 v[88:103], v[144:147], v[120:123], 0\n"
        "ds_read_b64 v[136:137], v8 offset:0\n"
        "v_exp_f32 v72, v72\n"
        "v_exp_f32 v73, v73\n"
        "s_setprio 1\n"
        "v_exp_f32 v74, v74\n"
        "v_exp_f32 v75, v75\n"
        "s_setprio 0\n"
        "v_smfmac_f32_32x32x32_bf16 v[24:39], v[156:159], v[64:71], v20\n"
        "ds_read_b64 v[138:139], v8 offset:8\n"
        "v_exp_f32 v76, v76\n"
        "v_exp_f32 v77, v77\n"
        "s_setprio 1\n"
        "v_exp_f32 v78, v78\n"
        "v_exp_f32 v79, v79\n"
        "s_setprio 0\n"
        "v_mfma_f32_32x32x16_bf16 v[88:103], v[148:151], v[124:127], v[88:103]\n"
        "ds_read_b64 v[140:141], v8 offset:32\n"
        "v_exp_f32 v80, v80\n"
        "v_exp_f32 v81, v81\n"
        "s_setprio 1\n"
        "v_exp_f32 v82, v82\n"
        "v_exp_f32 v83, v83\n"
        "s_setprio 0\n"
        "v_smfmac_f32_32x32x32_bf16 v[40:55], v[152:155], v[72:79], v20\n"
        "ds_read_b64 v[142:143], v12 offset:40\n"
        "v_exp_f32 v84, v84\n"
        "v_exp_f32 v85, v85\n"
        "s_setprio 1\n"
        "v_exp_f32 v86, v86\n"
        "v_exp_f32 v87, v87\n"
        "s_waitcnt lgkmcnt(4)\n"
        "s_setprio 0\n"
        "v_mfma_f32_32x32x16_bf16 v[104:119], v[144:147], v[128:131], 0\n"
        "ds_read_b64_tr_b16 v[152:153], v16 offset:0\n"
        "v_exp_f32 v88, v88\n"
        "v_exp_f32 v89, v89\n"
        "s_setprio 1\n"
        "v_exp_f32 v90, v90\n"
        "v_exp_f32 v91, v91\n"
        "s_setprio 0\n"
        "v_smfmac_f32_32x32x32_bf16 v[40:55], v[156:159], v[80:87], v20\n"
        "ds_read_b64_tr_b16 v[154:155], v16 offset:224\n"
        "v_exp_f32 v92, v92\n"
        "v_exp_f32 v93, v93\n"
        "s_setprio 1\n"
        "v_exp_f32 v94, v94\n"
        "v_exp_f32 v95, v95\n"
        "s_setprio 0\n"
        "v_mfma_f32_32x32x16_bf16 v[104:119], v[148:151], v[132:135], v[104:119]\n"
        "ds_read_b64_tr_b16 v[156:157], v16 offset:896\n"
        "v_exp_f32 v96, v96\n"
        "v_exp_f32 v97, v97\n"
        "s_setprio 1\n"
        "v_exp_f32 v98, v98\n"
        "v_exp_f32 v99, v99\n"
        "s_setprio 0\n"
        "v_smfmac_f32_32x32x32_bf16 v[24:39], v[160:163], v[88:95], v20\n"
        "ds_read_b64_tr_b16 v[158:159], v16 offset:1120\n"
        "v_exp_f32 v100, v100\n"
        "v_exp_f32 v101, v101\n"
        "s_setprio 1\n"
        "v_exp_f32 v102, v102\n"
        "v_exp_f32 v103, v103\n"
        "s_waitcnt lgkmcnt(4)\n"
        "s_setprio 0\n"
        "v_mfma_f32_32x32x16_bf16 v[56:71], v[136:139], v[120:123], 0\n"
        "ds_read_b64 v[144:145], v8 offset:1792\n"
        "v_exp_f32 v104, v104\n"
        "v_exp_f32 v105, v105\n"
        "s_setprio 1\n"
        "v_exp_f32 v106, v106\n"
        "v_exp_f32 v107, v107\n"
        "s_setprio 0\n"
        "v_smfmac_f32_32x32x32_bf16 v[24:39], v[164:167], v[96:103], v20\n"
        "ds_read_b64 v[146:147], v8 offset:1800\n"
        "v_exp_f32 v108, v108\n"
        "v_exp_f32 v109, v109\n"
        "s_setprio 1\n"
        "v_exp_f32 v110, v110\n"
        "v_exp_f32 v111, v111\n"
        "s_setprio 0\n"
        "v_mfma_f32_32x32x16_bf16 v[56:71], v[140:143], v[124:127], v[56:71]\n"
        "ds_read_b64 v[148:149], v8 offset:1824\n"
        "v_exp_f32 v112, v112\n"
        "v_exp_f32 v113, v113\n"
        "s_setprio 1\n"
        "v_exp_f32 v114, v114\n"
        "v_exp_f32 v115, v115\n"
        "s_setprio 0\n"
        "v_smfmac_f32_32x32x32_bf16 v[40:55], v[160:163], v[104:111], v20\n"
        "ds_read_b64 v[150:151], v12 offset:1832\n"
        "v_exp_f32 v116, v116\n"
        "v_exp_f32 v117, v117\n"
        "s_setprio 1\n"
        "v_exp_f32 v118, v118\n"
        "v_exp_f32 v119, v119\n"
        "s_sub_u32 s40, s40, 1\n"
        "s_cmp_lg_u32 s40, 0\n"
        "s_cbranch_scc1 1b\n"
        "; ---- epilogue: sum the two key halves, check the row sums, normalise, store ----\n"
        "s_nop 0\n"
        "v_smfmac_f32_32x32x32_bf16 v[40:55], v[164:167], v[112:119], v20\n"
        "ds_write_b128 v3, v[24:27] offset:0\n"
        "ds_write_b128 v3, v[28:31] offset:1024\n"
        "ds_write_b128 v3, v[32:35] offset:2048\n"
        "ds_write_b128 v3, v[36:39] offset:3072\n"
        "s_waitcnt lgkmcnt(0)\n"
        "s_barrier\n"
        "ds_read_b128 v[56:59], v4 offset:0\n"
        "ds_read_b128 v[60:63], v4 offset:1024\n"
        "ds_read_b128 v[64:67], v4 offset:2048\n"
        "ds_read_b128 v[68:71], v4 offset:3072\n"
        "s_nop 0\n"
        "s_waitcnt lgkmcnt(3)\n"
        "v_pk_add_f32 v[40:41], v[40:41], v[56:57]\n"
        "v_pk_add_f32 v[42:43], v[42:43], v[58:59]\n"
        "s_waitcnt lgkmcnt(2)\n"
        "v_pk_add_f32 v[44:45], v[44:45], v[60:61]\n"
        "v_pk_add_f32 v[46:47], v[46:47], v[62:63]\n"
        "s_waitcnt lgkmcnt(1)\n"
        "v_pk_add_f32 v[48:49], v[48:49], v[64:65]\n"
        "v_pk_add_f32 v[50:51], v[50:51], v[66:67]\n"
        "s_waitcnt lgkmcnt(0)\n"
        "v_pk_add_f32 v[52:53], v[52:53], v[68:69]\n"
        "v_pk_add_f32 v[54:55], v[54:55], v[70:71]\n"
        "v_mov_b32 v93, v52\n"
        "v_mov_b32 v94, v52\n"
        "v_maximum3_f32 v95, |v40|, |v41|, |v42|\n"
        "v_maximum3_f32 v96, |v43|, |v44|, |v45|\n"
        "v_permlane32_swap_b32 v93, v94\n"
        "v_maximum3_f32 v95, v95, |v46|, |v47|\n"
        "v_rcp_f32 v98, v94\n"
        "v_maximum3_f32 v96, v96, |v48|, |v49|\n"
        "v_maximum3_f32 v95, v95, |v50|, |v51|\n"
        "v_maximum3_f32 v96, v96, |v52|, |v53|\n"
        "v_maximum3_f32 v95, v95, |v54|, |v55|\n"
        "v_maximum3_f32 v95, v95, v96, v96\n"
        "s_mov_b32 s41, 0x35800000\n"
        "v_cmp_nle_f32 s[60:61], s41, v94\n"
        "s_mov_b32 s41, 0x71800000\n"
        "v_cmp_nge_f32 vcc, s41, v94\n"
        "s_mov_b32 s41, 0x7f800000\n"
        "v_cmp_ngt_f32 s[62:63], s41, v95\n"
        "v_pk_mul_f32 v[40:41], v[40:41], v[98:99] op_sel_hi:[1,0]\n"
        "v_pk_mul_f32 v[42:43], v[42:43], v[98:99] op_sel_hi:[1,0]\n"
        "v_pk_mul_f32 v[44:45], v[44:45], v[98:99] op_sel_hi:[1,0]\n"
        "v_pk_mul_f32 v[46:47], v[46:47], v[98:99] op_sel_hi:[1,0]\n"
        "v_pk_mul_f32 v[48:49], v[48:49], v[98:99] op_sel_hi:[1,0]\n"
        "v_pk_mul_f32 v[50:51], v[50:51], v[98:99] op_sel_hi:[1,0]\n"
        "v_pk_mul_f32 v[52:53], v[52:53], v[98:99] op_sel_hi:[1,0]\n"
        "v_pk_mul_f32 v[54:55], v[54:55], v[98:99] op_sel_hi:[1,0]\n"
        "s_or_b64 s[60:61], s[60:61], vcc\n"
        "s_or_b64 s[60:61], s[60:61], s[62:63]\n"
        "s_cmp_lg_u64 s[60:61], 0\n"
        "s_cselect_b32 %[bad], 1, 0\n"
        "s_cbranch_scc1 4f\n"
        "v_cvt_pk_bf16_f32 v56, v40, v41\n"
        "v_cvt_pk_bf16_f32 v57, v42, v43\n"
        "v_cvt_pk_bf16_f32 v58, v44, v45\n"
        "v_cvt_pk_bf16_f32 v59, v46, v47\n"
        "v_cvt_pk_bf16_f32 v60, v48, v49\n"
        "v_cvt_pk_bf16_f32 v61, v50, v51\n"
        "v_cvt_pk_bf16_f32 v62, v52, v53\n"
        "v_cvt_pk_bf16_f32 v63, v54, v55\n"
        "ds_write_b64 v6, v[56:57] offset:0\n"
        "ds_write_b64 v6, v[58:59] offset:16\n"
        "ds_write_b64 v6, v[60:61] offset:32\n"
        "s_mov_b32 exec_hi, 0\n"
        "ds_write_b64 v6, v[62:63] offset:48\n"
        "s_mov_b32 exec_hi, -1\n"
        "s_waitcnt lgkmcnt(0)\n"
        "ds_read_b128 v[72:75], v7\n"
        "ds_read_b128 v[76:79], v7 offset:1024\n"
        "s_waitcnt lgkmcnt(1)\n"
        "buffer_store_dwordx4 v[72:75], v5, %[srdo], %[sob] offen sc1\n"
        "s_mov_b32 exec_hi, 0xffff\n"
        "s_waitcnt lgkmcnt(0)\n"
        "buffer_store_dwordx4 v[76:79], v5, %[srdo], %[sob] offen offset:1024 sc1\n"
        "s_mov_b32 exec_hi, -1\n"
        "4:\n"
        "s_nop 1\n"
        ".Lattn_hot_end:\n"
        : [bad] "=&s"(bad)
        : [srdq] "s"(srdQ), [srdo] "s"(srdO), [sob] "s"((head * SEQ + rowB) * ROWB),
          [sg] "s"(lds0 + g * 2 * PIECE), [sl] "s"(lds0), [sw] "s"(lds0 + ONES + wid * 1024),
          [sxw] "s"(lds0 + wid * 4096), [sxr] "s"(lds0 + (wid ^ 4) * 4096), [sst] "s"(lds0 + 32768 + wid * 2048),
          [sqw] "s"(lds0 + 3 * SLOT + wid * 2048), [sqr] "s"(lds0 + 3 * SLOT + (wid ^ 4) * 2048)
        : "memory", "scc", "s40", "s41", "s70", "s71", "s72", "s60", "s61", "s62", "s63", "vcc", "v3", "v4", "v5", "v6", "v7", "v8", "v9", "v10", "v11", "v12", "v13", "v14", "v15", "v16", "v17", "v18", "v19", "v20", "v21", "v22", "v23", "v24", "v25", "v26", "v27", "v28", "v29", "v30", "v31", "v32", "v33", "v34", "v35", "v36", "v37", "v38", "v39", "v40", "v41", "v42", "v43", "v44", "v45", "v46", "v47", "v48", "v49", "v50", "v51", "v52", "v53", "v54", "v55", "v56", "v57", "v58", "v59", "v60", "v61", "v62", "v63", "v64", "v65", "v66", "v67", "v68", "v69", "v70", "v71", "v72", "v73", "v74", "v75", "v76", "v77", "v78", "v79", "v80", "v81", "v82", "v83", "v84", "v85", "v86", "v87", "v88", "v89", "v90", "v91", "v92", "v93", "v94", "v95", "v96", "v97", "v98", "v99", "v100", "v101", "v102", "v103", "v104", "v105", "v106", "v107", "v108", "v109", "v110", "v111", "v112", "v113", "v114", "v115", "v116", "v117", "v118", "v119", "v120", "v121", "v122", "v123", "v124", "v125", "v126", "v127", "v128", "v129", "v130", "v131", "v132", "v133", "v134", "v135", "v136", "v137", "v138", "v139", "v140", "v141", "v142", "v143", "v144", "v145", "v146", "v147", "v148", "v149", "v150", "v151", "v152", "v153", "v154", "v155", "v156", "v157", "v158", "v159", "v160", "v161", "v162", "v163", "v164", "v165", "v166", "v167");

    int ln;
    asm volatile("v_mbcnt_lo_u32_b32 %0, -1, 0\n\tv_mbcnt_hi_u32_b32 %0, -1, %0" : "=v"(ln));
    if (__builtin_expect(bad, 0)) {
        const f32x16 o = attn_rows_true_max(Q + (size_t)head * SEQ * HD, K + (size_t)head * SEQ * HD, V + (size_t)head * SEQ * HD, rowB, ln);
        const float inv = 1.0f / upper_half(o[12]);
        const int h = ln >> 5;
        char* op = (char*)O + ((size_t)head * SEQ + rowB + (ln & 31)) * ROWB + 8 * h;
#pragma unroll
        for (int i = 0; i < 4; ++i) {
            const u32x2 w = {cvtpk(o[4 * i] * inv, o[4 * i + 1] * inv), cvtpk(o[4 * i + 2] * inv, o[4 * i + 3] * inv)};
            if (i < 3 || h == 0) *(u32x2*)(op + 16 * i) = w;
        }
    }
}

extern "C" void kernel_launch(void* const* d_in, const int* in_sizes, int n_in, void* d_out, int out_size, void* d_ws,
                              size_t ws_size, hipStream_t stream) {
    (void)in_sizes; (void)n_in; (void)out_size; (void)d_ws; (void)ws_size;
    (void)hipFuncSetAttribute((const void*)standalone__fused_io_aware_attn__prompt_kernel,
                              hipFuncAttributeMaxDynamicSharedMemorySize, LDS_BYTES);
    hipLaunchKernelGGL(standalone__fused_io_aware_attn__prompt_kernel, dim3(NH * (SEQ / 256)), dim3(768), LDS_BYTES, stream,
                       (const u16*)d_in[0], (const u16*)d_in[1], (const u16*)d_in[2], (u16*)d_out);
}
```
